```python
import torch

def _softmax(x, axis=-1):
    return torch.softmax(x, dim=axis)
def _gelu(x):
    return torch.nn.functional.gelu(x, approximate="tanh")
def _silu(x):
    return torch.nn.functional.silu(x)
def _sigmoid(x):
    return torch.sigmoid(x)
def _layernorm(x, axis=-1, eps=1e-5):
    m = x.mean(dim=axis, keepdim=True)
    v = x.var(dim=axis, keepdim=True, unbiased=False)
    return (x - m) / torch.sqrt(v + eps)
def _scatter_add(tgt, idx, src, axis=0):
    return tgt.clone().index_add_(axis, idx, src)
def _segment_sum(src, seg, n):
    out = torch.zeros((n,) + src.shape[1:], dtype=src.dtype)
    return out.index_add_(0, seg, src)
def _pad(x, pw):
    flat = tuple(v for pair in reversed(pw) for v in pair)
    return torch.nn.functional.pad(x, flat)

kernel_name = 'interleave_layers__mha_block__glu_gated_swiglu__prompt'

def setup_inputs():

    _w = (lambda a: (lambda t: t.to(torch.bfloat16) if t.is_floating_point() else t)(torch.as_tensor(a)))
    def _A_setup():
        B, S, D, H = 1, 2048, 448, 16
        return {
            "x": _w((lambda *s: torch.randn(*s, dtype=torch.bfloat16) * 0.05)(B, S, D)),
            "wqkv": _w((lambda *s: torch.randn(*s, dtype=torch.bfloat16) * 0.05)(D, 3 * D)),
            "wo": _w((lambda *s: torch.randn(*s, dtype=torch.bfloat16) * 0.05)(D, D)),
        }
    _A_in = _A_setup()

    def _B_setup():
        B, S, D, F = 1, 2048, 448, 1792
        return {
            "x": _w((lambda *s: torch.randn(*s, dtype=torch.bfloat16) * 0.05)(B, S, D)),
            "w1": _w((lambda *s: torch.randn(*s, dtype=torch.bfloat16) * 0.4)(D, F) * (2.0 / D) ** 0.5),
            "w3": _w((lambda *s: torch.randn(*s, dtype=torch.bfloat16) * 0.4)(D, F) * (2.0 / D) ** 0.5),
            "w2": _w((lambda *s: torch.randn(*s, dtype=torch.bfloat16) * 0.4)(F, D) * (2.0 / F) ** 0.5),
        }
    _B_in = _B_setup()

    return {
        "A_x": _A_in["x"],
        "A_wqkv": _A_in["wqkv"],
        "A_wo": _A_in["wo"],
        "B_x": _B_in["x"],
        "B_w1": _B_in["w1"],
        "B_w3": _B_in["w3"],
        "B_w2": _B_in["w2"],
    }

def reference(A_x, A_wqkv, A_wo, B_x, B_w1, B_w3, B_w2):
    def _A_ref(x, wqkv, wo):
        B, S, D = x.shape
        H = 16
        Dh = D // H
        qkv = (lambda x, *d: x.permute(*d))((x @ wqkv).reshape(B, S, 3, H, Dh), 2, 0, 3, 1, 4)
        q, k, v = qkv[0], qkv[1], qkv[2]
        scores = torch.einsum("bhqd,bhkd->bhqk", q, k) * (Dh ** -0.5)
        p = _softmax(scores, axis=-1)
        o = torch.einsum("bhqk,bhkd->bhqd", p, v)
        return (lambda x, *d: x.permute(*d))(o, 0, 2, 1, 3).reshape(B, S, D) @ wo

    def _B_ref(x, w1, w3, w2):
        gate = torch.einsum("bsd,df->bsf", x, w1)
        up = torch.einsum("bsd,df->bsf", x, w3)
        h = _silu(gate) * up
        out = torch.einsum("bsf,fd->bsd", h, w2)
        return out

    x = A_x
    x = x + _A_ref(_layernorm(x, axis=-1), A_wqkv, A_wo)
    x = x + _B_ref(_layernorm(x, axis=-1), B_w1, B_w3, B_w2)
    x = x + _A_ref(_layernorm(x, axis=-1), A_wqkv, A_wo)
    x = x + _B_ref(_layernorm(x, axis=-1), B_w1, B_w3, B_w2)
    return x
```

```cpp
#include <hip/hip_runtime.h>
#include <hip/hip_bf16.h>
#include <stdint.h>

typedef unsigned short u16;
typedef __attribute__((ext_vector_type(8))) short bf16x8;
typedef __attribute__((ext_vector_type(4))) short s16x4;
typedef __attribute__((ext_vector_type(16))) float f32x16;
typedef __attribute__((ext_vector_type(4))) unsigned u32x4;
typedef __attribute__((ext_vector_type(2))) unsigned u32x2;
typedef __attribute__((address_space(3))) char lds_char;
typedef __attribute__((address_space(3))) s16x4 lds_s16x4;

constexpr int SEQ = 2048, DM = 448, NH = 16, DH = 28, FF = 1792;
constexpr float QSCALE = 0.18898223650461362f * 1.4426950408889634f;

__device__ __forceinline__ unsigned cvtpk(float lo, float hi) {
  unsigned r; asm("v_cvt_pk_bf16_f32 %0, %1, %2" : "=v"(r) : "v"(lo), "v"(hi)); return r;
}
__device__ __forceinline__ float bflo(unsigned u) { return __uint_as_float(u << 16); }
__device__ __forceinline__ float bfhi(unsigned u) { return __uint_as_float(u & 0xffff0000u); }
__device__ __forceinline__ s16x4 tr_read(const char* p) {
  return __builtin_bit_cast(s16x4, __builtin_amdgcn_ds_read_tr16_b64_v4i16((lds_s16x4*)p));
}
__device__ __forceinline__ unsigned off_b(unsigned row, unsigned ch) {
  return 256u * row + 16u * (ch ^ (((row & 3) << 2) | ((row >> 2) & 3)));
}

template <int BM, int BN, int KTOT, int AMODE, int BMODE, int EPI>
__global__ __launch_bounds__(256) void gemm_k(const u16* __restrict__ A, const u16* __restrict__ W0,
                                              const u16* __restrict__ W1, const u16* __restrict__ R,
                                              u16* __restrict__ out0, char* __restrict__ out1, int ldw) {
  constexpr int TM = BM / 64, TN = BN / 64;
  constexpr int PA = (AMODE == 1) ? (KTOT * 2 + 16) : 144;
  constexpr int ABYTES = (AMODE == 1) ? BM * PA : 2 * BM * PA;
  constexpr int NK = KTOT / 64;
  extern __shared__ __attribute__((aligned(16))) char smem[];
  char* As = smem;
  char* Bs = smem + ABYTES;
  const int tid = threadIdx.x, lane = tid & 63, wid = tid >> 6, wr = wid >> 1, wc = wid & 1;
  const int l32 = lane & 31, hh = lane >> 5;
  const int bn = blockIdx.x, bm0 = blockIdx.y * BM;

  constexpr int BCH = (BMODE == 1) ? 7 : (BN / 32);
  u32x4 breg[(BMODE == 1) ? 4 : BCH];
  constexpr int ACH = (AMODE == 1) ? 1 : BM / 32;
  u32x4 areg[ACH];

  auto load_b = [&](int kc) {
    const int k0 = kc * 64;
    if constexpr (BMODE == 0) {
#pragma unroll
      for (int i = 0; i < BCH; ++i) {
        const int idx = tid + 256 * i, k = idx / (BN / 8), ch = idx % (BN / 8);
        breg[i] = *(const u32x4*)(W0 + (size_t)(k0 + k) * ldw + bn * BN + 8 * ch);
      }
    } else if constexpr (BMODE == 1) {
      const int k = tid >> 2, hbl = tid & 3;
      const u32x2* src = (const u32x2*)(W0 + (size_t)(k0 + k) * ldw + (bn * 4 + hbl) * 28);
      u32x2 v[7];
#pragma unroll
      for (int i = 0; i < 7; ++i) v[i] = src[i];
      breg[0] = (u32x4){v[0].x, v[0].y, v[1].x, v[1].y};
      breg[1] = (u32x4){v[2].x, v[2].y, v[3].x, v[3].y};
      breg[2] = (u32x4){v[4].x, v[4].y, v[5].x, v[5].y};
      breg[3] = (u32x4){v[6].x, v[6].y, 0u, 0u};
    } else {
#pragma unroll
      for (int i = 0; i < BCH; ++i) {
        const int idx = tid + 256 * i, k = idx >> 4, ch = idx & 15;
        const u16* w = (ch < 8) ? W0 : W1;
        breg[i] = *(const u32x4*)(w + (size_t)(k0 + k) * ldw + bn * 64 + 8 * (ch & 7));
      }
    }
  };
  auto store_b = [&](char* buf) {
    if constexpr (BMODE == 1) {
      const int k = tid >> 2, hbl = tid & 3;
#pragma unroll
      for (int i = 0; i < 4; ++i) *(u32x4*)(buf + off_b(k, hbl * 4 + i)) = breg[i];
    } else {
#pragma unroll
      for (int i = 0; i < BCH; ++i) {
        const int idx = tid + 256 * i, k = idx / (BN / 8), ch = idx % (BN / 8);
        *(u32x4*)(buf + off_b(k, ch)) = breg[i];
      }
    }
  };
  auto load_a = [&](int kc) {
    if constexpr (AMODE == 0) {
#pragma unroll
      for (int i = 0; i < ACH; ++i) {
        const int idx = tid + 256 * i, row = idx >> 3, ch = idx & 7;
        areg[i] = *(const u32x4*)(A + (size_t)(bm0 + row) * KTOT + kc * 64 + 8 * ch);
      }
    }
  };
  auto store_a = [&](char* buf) {
    if constexpr (AMODE == 0) {
#pragma unroll
      for (int i = 0; i < ACH; ++i) {
        const int idx = tid + 256 * i, row = idx >> 3, ch = idx & 7;
        *(u32x4*)(buf + row * PA + ch * 16) = areg[i];
      }
    }
  };

  load_b(0);
  load_a(0);
  if constexpr (AMODE == 1) {
    const int sub = tid & 7;
#pragma unroll
    for (int p = 0; p < BM / 32; ++p) {
      const int row = p * 32 + (tid >> 3);
      const u16* src = A + (size_t)(bm0 + row) * KTOT;
      u32x4 v[7];
#pragma unroll
      for (int i = 0; i < 7; ++i) v[i] = *(const u32x4*)(src + 8 * (sub + 8 * i));
      float s = 0.f;
#pragma unroll
      for (int i = 0; i < 7; ++i)
#pragma unroll
        for (int j = 0; j < 4; ++j) s += bflo(v[i][j]) + bfhi(v[i][j]);
      s += __shfl_xor(s, 1); s += __shfl_xor(s, 2); s += __shfl_xor(s, 4);
      const float mean = s * (1.f / KTOT);
      float q = 0.f;
#pragma unroll
      for (int i = 0; i < 7; ++i)
#pragma unroll
        for (int j = 0; j < 4; ++j) { const float a = bflo(v[i][j]) - mean, b = bfhi(v[i][j]) - mean; q += a * a + b * b; }
      q += __shfl_xor(q, 1); q += __shfl_xor(q, 2); q += __shfl_xor(q, 4);
      const float rstd = 1.f / sqrtf(q * (1.f / KTOT) + 1e-5f);
#pragma unroll
      for (int i = 0; i < 7; ++i) {
        u32x4 o;
#pragma unroll
        for (int j = 0; j < 4; ++j) o[j] = cvtpk((bflo(v[i][j]) - mean) * rstd, (bfhi(v[i][j]) - mean) * rstd);
        *(u32x4*)(As + row * PA + 16 * (sub + 8 * i)) = o;
      }
    }
  }
  store_b(Bs);
  store_a(As);
  __syncthreads();

  f32x16 acc[TM][TN];
#pragma unroll
  for (int i = 0; i < TM; ++i)
#pragma unroll
    for (int j = 0; j < TN; ++j) acc[i][j] = (f32x16){};

  const int tq = (lane & 15) >> 2, tp = lane & 3, tblk = (lane >> 4) & 1;
  bool swapped = true;
  if constexpr (EPI == 0) swapped = (bn < 8);

#pragma unroll 1
  for (int kc = 0; kc < NK; ++kc) {
    if (kc + 1 < NK) { load_b(kc + 1); load_a(kc + 1); }
    const char* Bb = Bs + (kc & 1) * 16384;
    const char* Ab = (AMODE == 1) ? As : (As + (kc & 1) * BM * PA);
    const int kbase = (AMODE == 1) ? kc * 64 : 0;
#pragma unroll
    for (int s = 0; s < 4; ++s) {
      bf16x8 af[TM], bfr[TN];
#pragma unroll
      for (int i = 0; i < TM; ++i)
        af[i] = *(const bf16x8*)(Ab + (wr * (BM / 2) + 32 * i + l32) * PA + (kbase + 16 * s + 8 * hh) * 2);
#pragma unroll
      for (int j = 0; j < TN; ++j) {
        int cb;
        if constexpr (BMODE == 2) cb = wc + 2 * j; else cb = wc * TN + j;
        const unsigned r0 = 16 * s + 8 * hh + tq;
        const unsigned ch = 4 * cb + 2 * tblk + (tp >> 1);
        const s16x4 lo = tr_read(Bb + off_b(r0, ch) + 8 * (tp & 1));
        const s16x4 hi = tr_read(Bb + off_b(r0 + 4, ch) + 8 * (tp & 1));
        bfr[j] = (bf16x8){lo[0], lo[1], lo[2], lo[3], hi[0], hi[1], hi[2], hi[3]};
      }
      if (swapped) {
#pragma unroll
        for (int i = 0; i < TM; ++i)
#pragma unroll
          for (int j = 0; j < TN; ++j) acc[i][j] = __builtin_amdgcn_mfma_f32_32x32x16_bf16(bfr[j], af[i], acc[i][j], 0, 0, 0);
      } else {
#pragma unroll
        for (int i = 0; i < TM; ++i)
#pragma unroll
          for (int j = 0; j < TN; ++j) acc[i][j] = __builtin_amdgcn_mfma_f32_32x32x16_bf16(af[i], bfr[j], acc[i][j], 0, 0, 0);
      }
    }
    if (kc + 1 < NK) {
      store_b(Bs + ((kc + 1) & 1) * 16384);
      store_a(As + ((kc + 1) & 1) * BM * PA);
    }
    __syncthreads();
  }

#pragma unroll
  for (int i = 0; i < TM; ++i) {
    const int tok0 = bm0 + wr * (BM / 2) + 32 * i;
    if constexpr (EPI == 1) {
#pragma unroll
      for (int j = 0; j < TN; ++j) {
        const int tok = tok0 + l32;
#pragma unroll
        for (int g = 0; g < 4; ++g) {
          const int n4 = bn * BN + (wc * TN + j) * 32 + 8 * g + 4 * hh;
          const u32x2 rv = *(const u32x2*)(R + (size_t)tok * DM + n4);
          u32x2 o;
          o.x = cvtpk(bflo(rv.x) + acc[i][j][4 * g], bfhi(rv.x) + acc[i][j][4 * g + 1]);
          o.y = cvtpk(bflo(rv.y) + acc[i][j][4 * g + 2], bfhi(rv.y) + acc[i][j][4 * g + 3]);
          *(u32x2*)(out0 + (size_t)tok * DM + n4) = o;
        }
      }
    } else if constexpr (EPI == 2) {
      const int tok = tok0 + l32;
#pragma unroll
      for (int g = 0; g < 4; ++g) {
        const int f4 = bn * 64 + wc * 32 + 8 * g + 4 * hh;
        float hv[4];
#pragma unroll
        for (int e = 0; e < 4; ++e) {
          const float gt = acc[i][0][4 * g + e], up = acc[i][1][4 * g + e];
          hv[e] = gt / (1.f + __expf(-gt)) * up;
        }
        u32x2 o; o.x = cvtpk(hv[0], hv[1]); o.y = cvtpk(hv[2], hv[3]);
        *(u32x2*)(out0 + (size_t)tok * FF + f4) = o;
      }
    } else {
#pragma unroll
      for (int j = 0; j < TN; ++j) {
        const int hb = bn * 4 + wc * TN + j, which = hb >> 4, head = hb & 15;
        const int tile = tok0 >> 5;
        char* kvb = out1 + (size_t)head * 262144 + (size_t)tile * 4096;
        if (which == 0) {
          const int tok = tok0 + l32;
#pragma unroll
          for (int g = 0; g < 4; ++g) {
            u32x2 o;
            o.x = cvtpk(acc[i][j][4 * g] * QSCALE, acc[i][j][4 * g + 1] * QSCALE);
            o.y = cvtpk(acc[i][j][4 * g + 2] * QSCALE, acc[i][j][4 * g + 3] * QSCALE);
            *(u32x2*)(out0 + ((size_t)head * SEQ + tok) * 32 + 8 * g + 4 * hh) = o;
          }
        } else if (which == 1) {
#pragma unroll
          for (int g = 0; g < 4; ++g) {
            u32x2 o;
            o.x = cvtpk(acc[i][j][4 * g], acc[i][j][4 * g + 1]);
            o.y = cvtpk(acc[i][j][4 * g + 2], acc[i][j][4 * g + 3]);
            if (g == 3 && hh == 1) { o.x = 0x3f80u; o.y = 0u; }
            *(u32x2*)(kvb + (g >> 1) * 1024 + (l32 + 32 * (g & 1)) * 16 + 8 * hh) = o;
          }
        } else {
#pragma unroll
          for (int g = 0; g < 4; ++g) {
            u32x2 o;
            o.x = cvtpk(acc[i][j][4 * g], acc[i][j][4 * g + 1]);
            o.y = cvtpk(acc[i][j][4 * g + 2], acc[i][j][4 * g + 3]);
            if (l32 == 28) { o.x = 0x3f803f80u; o.y = 0x3f803f80u; }
            *(u32x2*)(kvb + 2048 + (g >> 1) * 1024 + (l32 + 32 * hh) * 16 + 8 * (g & 1)) = o;
          }
        }
      }
    }
  }
}

constexpr float ATHR = 8.f;
template <bool FIRST>
__device__ __forceinline__ void attn_tile(const char* tb, int lane, bf16x8 qf0, bf16x8& qf1, float& mapp, f32x16& o) {
  const int hh = lane >> 5;
  const bf16x8 k0 = *(const bf16x8*)(tb + lane * 16);
  const bf16x8 k1 = *(const bf16x8*)(tb + 1024 + lane * 16);
  f32x16 s = __builtin_amdgcn_mfma_f32_32x32x16_bf16(k0, qf0, (f32x16){}, 0, 0, 0);
  s = __builtin_amdgcn_mfma_f32_32x32x16_bf16(k1, qf1, s, 0, 0, 0);
  float t0 = fmaxf(fmaxf(s[0], s[1]), s[2]), t1 = fmaxf(fmaxf(s[3], s[4]), s[5]);
  t0 = fmaxf(fmaxf(t0, s[6]), s[7]); t1 = fmaxf(fmaxf(t1, s[8]), s[9]);
  t0 = fmaxf(fmaxf(t0, s[10]), s[11]); t1 = fmaxf(fmaxf(t1, s[12]), s[13]);
  t0 = fmaxf(fmaxf(t0, s[14]), s[15]);
  float tmax = fmaxf(t0, t1);
  {
    auto rr = __builtin_amdgcn_permlane32_swap(__float_as_uint(tmax), __float_as_uint(tmax), false, false);
    tmax = fmaxf(__uint_as_float(rr[0]), __uint_as_float(rr[1]));
  }
  if (FIRST || __builtin_expect(__any(tmax > ATHR), 0)) {
    const float mnew = FIRST ? bflo(cvtpk(tmax, tmax)) : bflo(cvtpk(mapp + fmaxf(tmax, 0.f), 0.f));
    const float delta = mnew - mapp;
    if (!FIRST) {
      const float alpha = __builtin_amdgcn_exp2f(-delta);
#pragma unroll
      for (int r = 0; r < 16; ++r) o[r] *= alpha;
    }
#pragma unroll
    for (int r = 0; r < 16; ++r) s[r] -= delta;
    mapp = mnew;
    if (hh) qf1[4] = (short)(cvtpk(-mnew, 0.f) & 0xffffu);
  }
#pragma unroll
  for (int r = 0; r < 16; ++r) s[r] = __builtin_amdgcn_exp2f(s[r]);
  u32x4 p0, p1;
#pragma unroll
  for (int r = 0; r < 4; ++r) { p0[r] = cvtpk(s[2 * r], s[2 * r + 1]); p1[r] = cvtpk(s[8 + 2 * r], s[9 + 2 * r]); }
  const bf16x8 v0 = *(const bf16x8*)(tb + 2048 + lane * 16);
  const bf16x8 v1 = *(const bf16x8*)(tb + 3072 + lane * 16);
  o = __builtin_amdgcn_mfma_f32_32x32x16_bf16(v0, __builtin_bit_cast(bf16x8, p0), o, 0, 0, 0);
  o = __builtin_amdgcn_mfma_f32_32x32x16_bf16(v1, __builtin_bit_cast(bf16x8, p1), o, 0, 0, 0);
}

__global__ __launch_bounds__(256) void attn_k(const u16* __restrict__ Qs, const char* __restrict__ KV, u16* __restrict__ O) {
  extern __shared__ __attribute__((aligned(16))) char smem[];
  const int tid = threadIdx.x, lane = tid & 63, wid = tid >> 6, l32 = lane & 31, hh = lane >> 5;
  const int head = blockIdx.x >> 4, qb = blockIdx.x & 15;
  const int q = qb * 128 + wid * 32 + l32;
  const char* kvh = KV + (size_t)head * 262144;
  auto stage = [&](int c, char* buf) {
#pragma unroll
    for (int i = 0; i < 8; ++i) {
      const int p = i * 4 + wid;
      __builtin_amdgcn_global_load_lds((const unsigned*)(kvh + (size_t)c * 32768 + p * 1024 + lane * 16),
                                       (unsigned*)(buf + p * 1024), 16, 0, 0);
    }
  };
  stage(0, smem);
  const bf16x8 qf0 = *(const bf16x8*)(Qs + ((size_t)head * SEQ + q) * 32 + 8 * hh);
  bf16x8 qf1 = *(const bf16x8*)(Qs + ((size_t)head * SEQ + q) * 32 + 16 + 8 * hh);
  f32x16 o = (f32x16){};
  float mapp = 0.f;
  __syncthreads();
#pragma unroll 1
  for (int c = 0; c < 8; ++c) {
    char* buf = smem + (c & 1) * 32768;
    if (c + 1 < 8) stage(c + 1, smem + ((c + 1) & 1) * 32768);
    if (c == 0) attn_tile<true>(buf, lane, qf0, qf1, mapp, o);
    else attn_tile<false>(buf, lane, qf0, qf1, mapp, o);
#pragma unroll 1
    for (int t = 1; t < 8; ++t) attn_tile<false>(buf + t * 4096, lane, qf0, qf1, mapp, o);
    __syncthreads();
  }
  float l = o[12];
  {
    auto rr = __builtin_amdgcn_permlane32_swap(__float_as_uint(l), __float_as_uint(l), false, false);
    l = __uint_as_float(rr[1]);
  }
  const float rl = 1.f / l;
  u16* orow = O + (size_t)q * DM + head * DH;
#pragma unroll
  for (int g = 0; g < 4; ++g) {
    if (g == 3 && hh == 1) continue;
    u32x2 w;
    w.x = cvtpk(o[4 * g] * rl, o[4 * g + 1] * rl);
    w.y = cvtpk(o[4 * g + 2] * rl, o[4 * g + 3] * rl);
    *(u32x2*)(orow + 8 * g + 4 * hh) = w;
  }
}

extern "C" void kernel_launch(void* const* d_in, const int* in_sizes, int n_in,
                              void* d_out, int out_size, void* d_ws, size_t ws_size,
                              hipStream_t stream) {
  const u16* x0 = (const u16*)d_in[0];
  const u16* wqkv = (const u16*)d_in[1];
  const u16* wo = (const u16*)d_in[2];
  const u16* w1 = (const u16*)d_in[4];
  const u16* w3 = (const u16*)d_in[5];
  const u16* w2 = (const u16*)d_in[6];
  char* ws = (char*)d_ws;
  u16* xa = (u16*)(ws);
  u16* xb = (u16*)(ws + (2u << 20));
  u16* Qs = (u16*)(ws + (4u << 20));
  char* KV = ws + (6u << 20);
  u16* Ob = (u16*)(ws + (10u << 20));
  u16* Hh = (u16*)(ws + (12u << 20));
  u16* out = (u16*)d_out;

  constexpr int LDS_LN = 128 * (448 * 2 + 16) + 32768;
  constexpr int LDS_P = 2 * 64 * 144 + 32768;
  auto k_qkv = gemm_k<128, 128, 448, 1, 1, 0>;
  auto k_oproj = gemm_k<64, 64, 448, 0, 0, 1>;
  auto k_gu = gemm_k<128, 128, 448, 1, 2, 2>;
  auto k_down = gemm_k<64, 64, 1792, 0, 0, 1>;
  hipFuncSetAttribute((const void*)k_qkv, hipFuncAttributeMaxDynamicSharedMemorySize, LDS_LN);
  hipFuncSetAttribute((const void*)k_gu, hipFuncAttributeMaxDynamicSharedMemorySize, LDS_LN);
  hipFuncSetAttribute((const void*)attn_k, hipFuncAttributeMaxDynamicSharedMemorySize, 65536);

  const u16* xin = x0;
  for (int layer = 0; layer < 2; ++layer) {
    u16* x1 = xa;
    u16* x2 = (layer == 0) ? xb : out;
    hipLaunchKernelGGL(k_qkv, dim3(12, 16), dim3(256), LDS_LN, stream, xin, wqkv, (const u16*)nullptr, (const u16*)nullptr, Qs, KV, 3 * DM);
    hipLaunchKernelGGL(attn_k, dim3(256), dim3(256), 65536, stream, Qs, (const char*)KV, Ob);
    hipLaunchKernelGGL(k_oproj, dim3(7, 32), dim3(256), LDS_P, stream, (const u16*)Ob, wo, (const u16*)nullptr, xin, x1, (char*)nullptr, DM);
    hipLaunchKernelGGL(k_gu, dim3(28, 16), dim3(256), LDS_LN, stream, (const u16*)x1, w1, w3, (const u16*)nullptr, Hh, (char*)nullptr, FF);
    hipLaunchKernelGGL(k_down, dim3(7, 32), dim3(256), LDS_P, stream, (const u16*)Hh, w2, (const u16*)nullptr, (const u16*)x1, x2, (char*)nullptr, DM);
    xin = x2;
  }
}
```

```cpp
#include <hip/hip_runtime.h>
#include <hip/hip_bf16.h>
#include <stdint.h>

typedef unsigned short u16;
typedef __attribute__((ext_vector_type(8))) short bf16x8;
typedef __attribute__((ext_vector_type(4))) short s16x4;
typedef __attribute__((ext_vector_type(16))) float f32x16;
typedef __attribute__((ext_vector_type(4))) unsigned u32x4;
typedef __attribute__((ext_vector_type(2))) unsigned u32x2;
typedef __attribute__((address_space(3))) s16x4 lds_s16x4;

constexpr int SEQ = 2048, DM = 448, NH = 16, DH = 28, FF = 1792;
constexpr float QSCALE = 0.18898223650461362f * 1.4426950408889634f;

__device__ __forceinline__ unsigned cvtpk(float lo, float hi) {
  unsigned r; asm("v_cvt_pk_bf16_f32 %0, %1, %2" : "=v"(r) : "v"(lo), "v"(hi)); return r;
}
__device__ __forceinline__ float bflo(unsigned u) { return __uint_as_float(u << 16); }
__device__ __forceinline__ float bfhi(unsigned u) { return __uint_as_float(u & 0xffff0000u); }
__device__ __forceinline__ s16x4 tr_read(const char* p) {
  return __builtin_bit_cast(s16x4, __builtin_amdgcn_ds_read_tr16_b64_v4i16((lds_s16x4*)p));
}
__device__ __forceinline__ void glds16(const void* g, void* l) {
  const unsigned lds_base = __builtin_amdgcn_readfirstlane((unsigned)(uintptr_t)l);
  unsigned sv;
  asm volatile("s_mov_b32 %0, m0\n\ts_mov_b32 m0, %2\n\ts_nop 0\n\tglobal_load_lds_dwordx4 %1, off\n\ts_mov_b32 m0, %0"
               : "=&s"(sv) : "v"(g), "s"(lds_base) : "memory");
}
template <int N> __device__ __forceinline__ void wait_bar() {
  asm volatile("s_waitcnt vmcnt(%0) lgkmcnt(0)\n\ts_barrier" ::"i"(N) : "memory");
}
__device__ __forceinline__ unsigned swz_b256(unsigned row) { return ((row & 3) << 2) | ((row >> 2) & 3); }
__device__ __forceinline__ unsigned swz_b128(unsigned row) { return ((row >> 1) & 1) << 2; }
__device__ __forceinline__ unsigned swz_a128(unsigned row) { return (row >> 1) & 7; }

template <int BM, int BN, int KTOT, int NS, int WM, int WN, int AMODE, int BMODE, int EPI>
__global__ __launch_bounds__(64 * WM * WN) void gemm_k(const u16* __restrict__ A, const u16* __restrict__ W0,
                                                      const u16* __restrict__ W1, const u16* __restrict__ R,
                                                      u16* __restrict__ out0, char* __restrict__ out1, int ldw, int ntn) {
  constexpr int NT = 64 * WM * WN, NWV = WM * WN;
  constexpr int TM = BM / WM / 32, TN = BN / WN / 32;
  constexpr int NK = KTOT / 64;
  constexpr int PAR = KTOT * 2 + 16;
  constexpr int ASTG = (AMODE == 0) ? BM * 128 : 0;
  constexpr int BSTG = 64 * BN * 2;
  constexpr int STG = ASTG + BSTG;
  constexpr int APIECES = ASTG / 1024, BPIECES = BSTG / 1024;
  constexpr int LPS = (APIECES + BPIECES + NWV - 1) / NWV;
  static_assert((APIECES + BPIECES) % NWV == 0, "pieces must divide evenly over waves");
  extern __shared__ __attribute__((aligned(16))) char smem[];
  char* ring = smem;
  char* Ares = smem + NS * STG;
  const int tid = threadIdx.x, lane = tid & 63;
  const int wid = __builtin_amdgcn_readfirstlane(tid >> 6), wr = wid / WN, wc = wid % WN;
  const int l32 = lane & 31, hh = lane >> 5;
  const int nwg = gridDim.x;
  const int lin = (blockIdx.x & 7) * (nwg >> 3) + (blockIdx.x >> 3);
  const int bn = lin % ntn, bm0 = (lin / ntn) * BM;

  auto issue = [&](int kc) {
    char* slot = ring + (kc % NS) * STG;
#pragma unroll
    for (int i = 0; i < LPS; ++i) {
      const int p = i * NWV + wid;
      if (p < APIECES) {
        const int row = p * 8 + (lane >> 3), ph = lane & 7, ch = ph ^ swz_a128(row);
        glds16(A + (size_t)(bm0 + row) * KTOT + kc * 64 + 8 * ch, slot + p * 1024);
      } else {
        const int q = p - APIECES;
        if constexpr (BN == 64) {
          const int row = q * 8 + (lane >> 3), ph = lane & 7, ch = ph ^ swz_b128(row);
          glds16(W0 + (size_t)(kc * 64 + row) * ldw + bn * BN + 8 * ch, slot + ASTG + q * 1024);
        } else {
          const int panel = q / 16, row = (q % 16) * 4 + (lane >> 4), ph = lane & 15, ch = ph ^ swz_b256(row);
          if constexpr (BMODE == 2) {
            const u16* w = (ch < 8) ? W0 : W1;
            glds16(w + (size_t)(kc * 64 + row) * ldw + bn * 64 + 8 * (ch & 7), slot + ASTG + q * 1024);
          } else {
            glds16(W0 + (size_t)(kc * 64 + row) * ldw + bn * BN + panel * 128 + 8 * ch, slot + ASTG + q * 1024);
          }
        }
      }
    }
  };

#pragma unroll
  for (int s = 0; s < NS - 1; ++s) issue(s);

  if constexpr (AMODE == 1) {
    const int sub = tid & 7;
    constexpr int NP = BM / (NT / 8);
    u32x4 vv[NP][7];
#pragma unroll
    for (int p = 0; p < NP; ++p)
#pragma unroll
      for (int i = 0; i < 7; ++i) vv[p][i] = *(const u32x4*)(A + (size_t)(bm0 + p * (NT / 8) + (tid >> 3)) * KTOT + 8 * (sub + 8 * i));
    asm volatile("" ::: "memory");
#pragma unroll
    for (int p = 0; p < NP; ++p) {
      const int row = p * (NT / 8) + (tid >> 3);
      u32x4* v = vv[p];
      float s = 0.f;
#pragma unroll
      for (int i = 0; i < 7; ++i)
#pragma unroll
        for (int j = 0; j < 4; ++j) s += bflo(v[i][j]) + bfhi(v[i][j]);
      s += __shfl_xor(s, 1); s += __shfl_xor(s, 2); s += __shfl_xor(s, 4);
      const float mean = s * (1.f / KTOT);
      float q = 0.f;
#pragma unroll
      for (int i = 0; i < 7; ++i)
#pragma unroll
        for (int j = 0; j < 4; ++j) { const float a = bflo(v[i][j]) - mean, b = bfhi(v[i][j]) - mean; q += a * a + b * b; }
      q += __shfl_xor(q, 1); q += __shfl_xor(q, 2); q += __shfl_xor(q, 4);
      const float rstd = 1.f / sqrtf(q * (1.f / KTOT) + 1e-5f);
#pragma unroll
      for (int i = 0; i < 7; ++i) {
        u32x4 o;
#pragma unroll
        for (int j = 0; j < 4; ++j) o[j] = cvtpk((bflo(v[i][j]) - mean) * rstd, (bfhi(v[i][j]) - mean) * rstd);
        *(u32x4*)(Ares + row * PAR + 16 * (sub + 8 * i)) = o;
      }
    }
  }

  f32x16 acc[TM][TN];
#pragma unroll
  for (int i = 0; i < TM; ++i)
#pragma unroll
    for (int j = 0; j < TN; ++j) acc[i][j] = (f32x16){};

  const int tq = (lane & 15) >> 2, tp = lane & 3, tblk = (lane >> 4) & 1;
  bool swapped = true;
  if constexpr (EPI == 0) swapped = (bn < 14);

  auto compute = [&](int kc) {
    const char* slot = ring + (kc % NS) * STG;
    const char* Bb = slot + ASTG;
#pragma unroll
    for (int s = 0; s < 4; ++s) {
      bf16x8 af[TM], bfr[TN];
#pragma unroll
      for (int i = 0; i < TM; ++i) {
        const int row = wr * (BM / WM) + 32 * i + l32;
        if constexpr (AMODE == 1) af[i] = *(const bf16x8*)(Ares + row * PAR + (kc * 64 + 16 * s + 8 * hh) * 2);
        else af[i] = *(const bf16x8*)(slot + row * 128 + 16 * ((2 * s + hh) ^ swz_a128(row)));
      }
#pragma unroll
      for (int j = 0; j < TN; ++j) {
        int cb;
        if constexpr (BMODE == 2) cb = wc + 2 * j; else cb = wc * TN + j;
        const unsigned r0 = 16 * s + 8 * hh + tq;
        s16x4 lo, hi;
        if constexpr (BN == 64) {
          const unsigned ch = 4 * cb + 2 * tblk + (tp >> 1);
          lo = tr_read(Bb + 128 * r0 + 16 * (ch ^ swz_b128(r0)) + 8 * (tp & 1));
          hi = tr_read(Bb + 128 * (r0 + 4) + 16 * (ch ^ swz_b128(r0 + 4)) + 8 * (tp & 1));
        } else {
          const unsigned ch = 4 * (cb & 3) + 2 * tblk + (tp >> 1);
          const char* pb = Bb + (cb >> 2) * 16384;
          lo = tr_read(pb + 256 * r0 + 16 * (ch ^ swz_b256(r0)) + 8 * (tp & 1));
          hi = tr_read(pb + 256 * (r0 + 4) + 16 * (ch ^ swz_b256(r0 + 4)) + 8 * (tp & 1));
        }
        bfr[j] = (bf16x8){lo[0], lo[1], lo[2], lo[3], hi[0], hi[1], hi[2], hi[3]};
      }
      if (swapped) {
#pragma unroll
        for (int i = 0; i < TM; ++i)
#pragma unroll
          for (int j = 0; j < TN; ++j) acc[i][j] = __builtin_amdgcn_mfma_f32_32x32x16_bf16(bfr[j], af[i], acc[i][j], 0, 0, 0);
      } else {
#pragma unroll
        for (int i = 0; i < TM; ++i)
#pragma unroll
          for (int j = 0; j < TN; ++j) acc[i][j] = __builtin_amdgcn_mfma_f32_32x32x16_bf16(af[i], bfr[j], acc[i][j], 0, 0, 0);
      }
    }
  };

  constexpr int STEADY = NK - (NS - 1);
#pragma unroll 1
  for (int kc = 0; kc < STEADY; ++kc) {
    wait_bar<(NS - 2) * LPS>();
    issue(kc + NS - 1);
    compute(kc);
  }
#pragma unroll
  for (int kc = (STEADY > 0 ? STEADY : 0); kc < NK; ++kc) {
    wait_bar<0>();
    compute(kc);
  }

#pragma unroll
  for (int i = 0; i < TM; ++i) {
    const int tok0 = bm0 + wr * (BM / WM) + 32 * i;
    if constexpr (EPI == 1) {
#pragma unroll
      for (int j = 0; j < TN; ++j) {
        const int tok = tok0 + l32;
#pragma unroll
        for (int g = 0; g < 4; ++g) {
          const int n4 = bn * BN + (wc * TN + j) * 32 + 8 * g + 4 * hh;
          const u32x2 rv = *(const u32x2*)(R + (size_t)tok * DM + n4);
          u32x2 o;
          o.x = cvtpk(bflo(rv.x) + acc[i][j][4 * g], bfhi(rv.x) + acc[i][j][4 * g + 1]);
          o.y = cvtpk(bflo(rv.y) + acc[i][j][4 * g + 2], bfhi(rv.y) + acc[i][j][4 * g + 3]);
          *(u32x2*)(out0 + (size_t)tok * DM + n4) = o;
        }
      }
    } else if constexpr (EPI == 2) {
      const int tok = tok0 + l32;
#pragma unroll
      for (int g = 0; g < 4; ++g) {
        const int f4 = bn * 64 + wc * 32 + 8 * g + 4 * hh;
        float hv[4];
#pragma unroll
        for (int e = 0; e < 4; ++e) {
          const float gt = acc[i][0][4 * g + e], up = acc[i][1][4 * g + e];
          hv[e] = gt / (1.f + __expf(-gt)) * up;
        }
        u32x2 o; o.x = cvtpk(hv[0], hv[1]); o.y = cvtpk(hv[2], hv[3]);
        *(u32x2*)(out0 + (size_t)tok * FF + f4) = o;
      }
    } else {
      const int which = bn / 7, tile = tok0 >> 5;
#pragma unroll
      for (int j = 0; j < TN; ++j) {
        const int nb = (bn % 7) * 64 + (wc * TN + j) * 32;
        if (which < 2) {
          const int tok = tok0 + l32;
#pragma unroll
          for (int g = 0; g < 4; ++g) {
            const int n4 = nb + 8 * g + 4 * hh, head = n4 / DH, d = n4 - head * DH;
            u32x2 o;
            if (which == 0) {
              o.x = cvtpk(acc[i][j][4 * g] * QSCALE, acc[i][j][4 * g + 1] * QSCALE);
              o.y = cvtpk(acc[i][j][4 * g + 2] * QSCALE, acc[i][j][4 * g + 3] * QSCALE);
              u16* qp = out0 + ((size_t)head * SEQ + tok) * 32 + d;
              *(u32x2*)qp = o;
              if (d == 24) *(u32x2*)(qp + 4) = (u32x2){0u, 0u};
            } else {
              o.x = cvtpk(acc[i][j][4 * g], acc[i][j][4 * g + 1]);
              o.y = cvtpk(acc[i][j][4 * g + 2], acc[i][j][4 * g + 3]);
              char* kp = out1 + (size_t)head * 262144 + (size_t)tile * 4096 + (d >> 4) * 1024 + (l32 + 32 * ((d >> 3) & 1)) * 16 + 2 * (d & 7);
              *(u32x2*)kp = o;
              if (d == 24) *(u32x2*)(kp + 8) = (u32x2){0x3f80u, 0u};
            }
          }
        } else {
          const int n = nb + l32, head = n / DH, d = n - head * DH;
          char* vb = out1 + (size_t)head * 262144 + (size_t)tile * 4096 + 2048;
#pragma unroll
          for (int g = 0; g < 4; ++g) {
            u32x2 o;
            o.x = cvtpk(acc[i][j][4 * g], acc[i][j][4 * g + 1]);
            o.y = cvtpk(acc[i][j][4 * g + 2], acc[i][j][4 * g + 3]);
            char* vp = vb + (g >> 1) * 1024 + (d + 32 * hh) * 16 + 8 * (g & 1);
            *(u32x2*)vp = o;
            if (d == 27) {
              *(u32x2*)(vp + 16) = (u32x2){0x3f803f80u, 0x3f803f80u};
              *(u32x2*)(vp + 32) = (u32x2){0u, 0u};
              *(u32x2*)(vp + 48) = (u32x2){0u, 0u};
              *(u32x2*)(vp + 64) = (u32x2){0u, 0u};
            }
          }
        }
      }
    }
  }
}

constexpr float ATHR = 8.f;
template <bool FIRST>
__device__ __forceinline__ void attn_tile(const char* tb, int lane, bf16x8 qf0, bf16x8& qf1, float& mapp, f32x16& o) {
  const int hh = lane >> 5;
  const bf16x8 k0 = *(const bf16x8*)(tb + lane * 16);
  const bf16x8 k1 = *(const bf16x8*)(tb + 1024 + lane * 16);
  f32x16 s = __builtin_amdgcn_mfma_f32_32x32x16_bf16(k0, qf0, (f32x16){}, 0, 0, 0);
  s = __builtin_amdgcn_mfma_f32_32x32x16_bf16(k1, qf1, s, 0, 0, 0);
  float t0 = fmaxf(fmaxf(s[0], s[1]), s[2]), t1 = fmaxf(fmaxf(s[3], s[4]), s[5]);
  t0 = fmaxf(fmaxf(t0, s[6]), s[7]); t1 = fmaxf(fmaxf(t1, s[8]), s[9]);
  t0 = fmaxf(fmaxf(t0, s[10]), s[11]); t1 = fmaxf(fmaxf(t1, s[12]), s[13]);
  t0 = fmaxf(fmaxf(t0, s[14]), s[15]);
  float tmax = fmaxf(t0, t1);
  {
    auto rr = __builtin_amdgcn_permlane32_swap(__float_as_uint(tmax), __float_as_uint(tmax), false, false);
    tmax = fmaxf(__uint_as_float(rr[0]), __uint_as_float(rr[1]));
  }
  if (FIRST || __builtin_expect(__any(tmax > ATHR), 0)) {
    const float mnew = FIRST ? bflo(cvtpk(tmax, tmax)) : bflo(cvtpk(mapp + fmaxf(tmax, 0.f), 0.f));
    const float delta = mnew - mapp;
    if (!FIRST) {
      const float alpha = __builtin_amdgcn_exp2f(-delta);
#pragma unroll
      for (int r = 0; r < 16; ++r) o[r] *= alpha;
    }
#pragma unroll
    for (int r = 0; r < 16; ++r) s[r] -= delta;
    mapp = mnew;
    if (hh) qf1[4] = (short)(cvtpk(-mnew, 0.f) & 0xffffu);
  }
#pragma unroll
  for (int r = 0; r < 16; ++r) s[r] = __builtin_amdgcn_exp2f(s[r]);
  u32x4 p0, p1;
#pragma unroll
  for (int r = 0; r < 4; ++r) { p0[r] = cvtpk(s[2 * r], s[2 * r + 1]); p1[r] = cvtpk(s[8 + 2 * r], s[9 + 2 * r]); }
  const bf16x8 v0 = *(const bf16x8*)(tb + 2048 + lane * 16);
  const bf16x8 v1 = *(const bf16x8*)(tb + 3072 + lane * 16);
  o = __builtin_amdgcn_mfma_f32_32x32x16_bf16(v0, __builtin_bit_cast(bf16x8, p0), o, 0, 0, 0);
  o = __builtin_amdgcn_mfma_f32_32x32x16_bf16(v1, __builtin_bit_cast(bf16x8, p1), o, 0, 0, 0);
}

constexpr int ANS = 3;
__global__ __launch_bounds__(512) void attn_k(const u16* __restrict__ Qs, const char* __restrict__ KV, u16* __restrict__ O) {
  extern __shared__ __attribute__((aligned(16))) char smem[];
  const int tid = threadIdx.x, lane = tid & 63, l32 = lane & 31, hh = lane >> 5;
  const int wid = __builtin_amdgcn_readfirstlane(tid >> 6), qt = wid & 3, kh = wid >> 2;
  const int head = blockIdx.x >> 4, qb = blockIdx.x & 15;
  const int q = qb * 128 + qt * 32 + l32;
  const char* kvh = KV + (size_t)head * 262144;
  auto stage = [&](int c) {
    char* buf = smem + (c % ANS) * 32768;
#pragma unroll
    for (int i = 0; i < 4; ++i) {
      const int p = i * 8 + wid;
      glds16(kvh + (size_t)(p >> 4) * 131072 + (size_t)c * 16384 + (p & 15) * 1024 + lane * 16, buf + p * 1024);
    }
  };
  stage(0);
  stage(1);
  const bf16x8 qf0 = *(const bf16x8*)(Qs + ((size_t)head * SEQ + q) * 32 + 8 * hh);
  bf16x8 qf1 = *(const bf16x8*)(Qs + ((size_t)head * SEQ + q) * 32 + 16 + 8 * hh);
  f32x16 o = (f32x16){};
  float mapp = 0.f;
#pragma unroll 1
  for (int c = 0; c < 8; ++c) {
    if (c < 7) wait_bar<4>(); else wait_bar<0>();
    if (c + 2 < 8) stage(c + 2);
    const char* buf = smem + (c % ANS) * 32768 + kh * 16384;
    if (c == 0) attn_tile<true>(buf, lane, qf0, qf1, mapp, o);
    else attn_tile<false>(buf, lane, qf0, qf1, mapp, o);
#pragma unroll 1
    for (int t = 1; t < 4; ++t) attn_tile<false>(buf + t * 4096, lane, qf0, qf1, mapp, o);
  }
  __syncthreads();
  float* mbuf = (float*)smem;
  if (kh == 1) {
#pragma unroll
    for (int r = 0; r < 16; ++r) mbuf[(qt * 17 + r) * 64 + lane] = o[r];
    mbuf[(qt * 17 + 16) * 64 + lane] = mapp;
  }
  __syncthreads();
  if (kh == 0) {
    const float m1 = mbuf[(qt * 17 + 16) * 64 + lane];
    const float mm = fmaxf(mapp, m1);
    const float a0 = __builtin_amdgcn_exp2f(mapp - mm), a1 = __builtin_amdgcn_exp2f(m1 - mm);
#pragma unroll
    for (int r = 0; r < 16; ++r) o[r] = o[r] * a0 + mbuf[(qt * 17 + r) * 64 + lane] * a1;
    float l = o[12];
    {
      auto rr = __builtin_amdgcn_permlane32_swap(__float_as_uint(l), __float_as_uint(l), false, false);
      l = __uint_as_float(rr[1]);
    }
    const float rl = 1.f / l;
    u16* orow = O + (size_t)q * DM + head * DH;
#pragma unroll
    for (int g = 0; g < 4; ++g) {
      if (g == 3 && hh == 1) continue;
      u32x2 w;
      w.x = cvtpk(o[4 * g] * rl, o[4 * g + 1] * rl);
      w.y = cvtpk(o[4 * g + 2] * rl, o[4 * g + 3] * rl);
      *(u32x2*)(orow + 8 * g + 4 * hh) = w;
    }
  }
}

extern "C" void kernel_launch(void* const* d_in, const int* in_sizes, int n_in,
                              void* d_out, int out_size, void* d_ws, size_t ws_size,
                              hipStream_t stream) {
  const u16* x0 = (const u16*)d_in[0];
  const u16* wqkv = (const u16*)d_in[1];
  const u16* wo = (const u16*)d_in[2];
  const u16* w1 = (const u16*)d_in[4];
  const u16* w3 = (const u16*)d_in[5];
  const u16* w2 = (const u16*)d_in[6];
  char* ws = (char*)d_ws;
  u16* xa = (u16*)(ws);
  u16* xb = (u16*)(ws + (2u << 20));
  u16* Qs = (u16*)(ws + (4u << 20));
  char* KV = ws + (6u << 20);
  u16* Ob = (u16*)(ws + (10u << 20));
  u16* Hh = (u16*)(ws + (12u << 20));
  u16* out = (u16*)d_out;

  constexpr int LDS_QKV = 4 * 8192 + 128 * (448 * 2 + 16);
  constexpr int LDS_GU = 2 * 16384 + 128 * (448 * 2 + 16);
  constexpr int LDS_P = 4 * 16384;
  auto k_qkv = gemm_k<128, 64, 448, 4, 2, 2, 1, 0, 0>;
  auto k_oproj = gemm_k<64, 64, 448, 4, 2, 2, 0, 0, 1>;
  auto k_gu = gemm_k<128, 128, 448, 2, 2, 2, 1, 2, 2>;
  auto k_down = gemm_k<64, 64, 1792, 4, 2, 2, 0, 0, 1>;
  (void)hipFuncSetAttribute((const void*)k_qkv, hipFuncAttributeMaxDynamicSharedMemorySize, LDS_QKV);
  (void)hipFuncSetAttribute((const void*)k_gu, hipFuncAttributeMaxDynamicSharedMemorySize, LDS_GU);
  (void)hipFuncSetAttribute((const void*)k_oproj, hipFuncAttributeMaxDynamicSharedMemorySize, LDS_P);
  (void)hipFuncSetAttribute((const void*)k_down, hipFuncAttributeMaxDynamicSharedMemorySize, LDS_P);
  (void)hipFuncSetAttribute((const void*)attn_k, hipFuncAttributeMaxDynamicSharedMemorySize, ANS * 32768);

  const u16* xin = x0;
  for (int layer = 0; layer < 2; ++layer) {
    u16* x1 = xa;
    u16* x2 = (layer == 0) ? xb : out;
    hipLaunchKernelGGL(k_qkv, dim3(21 * 16), dim3(256), LDS_QKV, stream, xin, wqkv, (const u16*)nullptr, (const u16*)nullptr, Qs, KV, 3 * DM, 21);
    hipLaunchKernelGGL(attn_k, dim3(256), dim3(512), ANS * 32768, stream, Qs, (const char*)KV, Ob);
    hipLaunchKernelGGL(k_oproj, dim3(7 * 32), dim3(256), LDS_P, stream, (const u16*)Ob, wo, (const u16*)nullptr, xin, x1, (char*)nullptr, DM, 7);
    hipLaunchKernelGGL(k_gu, dim3(28 * 16), dim3(256), LDS_GU, stream, (const u16*)x1, w1, w3, (const u16*)nullptr, Hh, (char*)nullptr, FF, 28);
    hipLaunchKernelGGL(k_down, dim3(7 * 32), dim3(256), LDS_P, stream, (const u16*)Hh, w2, (const u16*)nullptr, (const u16*)x1, x2, (char*)nullptr, DM, 7);
    xin = x2;
  }
}
```

```cpp
#include <hip/hip_runtime.h>
#include <hip/hip_bf16.h>
#include <stdint.h>
#include <type_traits>

typedef unsigned short u16;
typedef __attribute__((ext_vector_type(8))) short bf16x8;
typedef __attribute__((ext_vector_type(4))) short s16x4;
typedef __attribute__((ext_vector_type(16))) float f32x16;
typedef __attribute__((ext_vector_type(4))) unsigned u32x4;
typedef __attribute__((ext_vector_type(2))) unsigned u32x2;
typedef __attribute__((address_space(3))) s16x4 lds_s16x4;

constexpr int SEQ = 2048, DM = 448, NH = 16, DH = 28, FF = 1792;
constexpr float QSCALE = 0.18898223650461362f * 1.4426950408889634f;

__device__ __forceinline__ unsigned cvtpk(float lo, float hi) {
  unsigned r; asm("v_cvt_pk_bf16_f32 %0, %1, %2" : "=v"(r) : "v"(lo), "v"(hi)); return r;
}
__device__ __forceinline__ float bflo(unsigned u) { return __uint_as_float(u << 16); }
__device__ __forceinline__ float bfhi(unsigned u) { return __uint_as_float(u & 0xffff0000u); }
__device__ __forceinline__ s16x4 tr_read(const char* p) {
  return __builtin_bit_cast(s16x4, __builtin_amdgcn_ds_read_tr16_b64_v4i16((lds_s16x4*)p));
}
__device__ __forceinline__ void glds16(const void* g, void* l) {
  const unsigned lds_base = __builtin_amdgcn_readfirstlane((unsigned)(uintptr_t)l);
  unsigned sv;
  asm volatile("s_mov_b32 %0, m0\n\ts_mov_b32 m0, %2\n\ts_nop 0\n\tglobal_load_lds_dwordx4 %1, off\n\ts_mov_b32 m0, %0"
               : "=&s"(sv) : "v"(g), "s"(lds_base) : "memory");
}
template <int N> __device__ __forceinline__ void wait_bar() {
  asm volatile("s_waitcnt vmcnt(%0) lgkmcnt(0)\n\ts_barrier" ::"i"(N) : "memory");
}
__device__ __forceinline__ unsigned swz_b256(unsigned row) { return ((row & 3) << 2) | ((row >> 2) & 3); }
__device__ __forceinline__ unsigned swz_b128(unsigned row) { return ((row >> 1) & 1) << 2; }
__device__ __forceinline__ unsigned swz_a128(unsigned row) { return (row >> 1) & 7; }

template <int BM, int BN, int KTOT, int NS, int WM, int WN, int AMODE, int BMODE, int EPI>
__global__ __launch_bounds__(64 * WM * WN) void gemm_k(const u16* __restrict__ A, const u16* __restrict__ W0,
                                                      const u16* __restrict__ W1, const u16* __restrict__ R,
                                                      u16* __restrict__ out0, char* __restrict__ out1, int ldw, int ntn) {
  constexpr int NT = 64 * WM * WN, NWV = WM * WN;
  constexpr int TM = BM / WM / 32, TN = BN / WN / 32;
  constexpr int NK = KTOT / 64;
  constexpr int PAR = KTOT * 2 + 16;
  constexpr int ASTG = (AMODE == 0) ? BM * 128 : 0;
  constexpr int BSTG = 64 * BN * 2;
  constexpr int STG = ASTG + BSTG;
  constexpr int APIECES = ASTG / 1024, BPIECES = BSTG / 1024;
  constexpr int LPS = (APIECES + BPIECES + NWV - 1) / NWV;
  static_assert((APIECES + BPIECES) % NWV == 0, "pieces must divide evenly over waves");
  extern __shared__ __attribute__((aligned(16))) char smem[];
  char* ring = smem;
  char* Ares = smem + NS * STG;
  const int tid = threadIdx.x, lane = tid & 63;
  const int wid = __builtin_amdgcn_readfirstlane(tid >> 6), wr = wid / WN, wc = wid % WN;
  const int l32 = lane & 31, hh = lane >> 5;
  const int nwg = gridDim.x;
  const int lin = (blockIdx.x & 7) * (nwg >> 3) + (blockIdx.x >> 3);
  const int bn = lin % ntn, bm0 = (lin / ntn) * BM;

  auto issue = [&](int kc) {
    char* slot = ring + (kc % NS) * STG;
#pragma unroll
    for (int i = 0; i < LPS; ++i) {
      const int p = i * NWV + wid;
      if (p < APIECES) {
        const int row = p * 8 + (lane >> 3), ph = lane & 7, ch = ph ^ swz_a128(row);
        glds16(A + (size_t)(bm0 + row) * KTOT + kc * 64 + 8 * ch, slot + p * 1024);
      } else {
        const int q = p - APIECES;
        if constexpr (BN == 64) {
          const int row = q * 8 + (lane >> 3), ph = lane & 7, ch = ph ^ swz_b128(row);
          glds16(W0 + (size_t)(kc * 64 + row) * ldw + bn * BN + 8 * ch, slot + ASTG + q * 1024);
        } else {
          const int panel = q / 16, row = (q % 16) * 4 + (lane >> 4), ph = lane & 15, ch = ph ^ swz_b256(row);
          if constexpr (BMODE == 2) {
            const u16* w = (ch < 8) ? W0 : W1;
            glds16(w + (size_t)(kc * 64 + row) * ldw + bn * 64 + 8 * (ch & 7), slot + ASTG + q * 1024);
          } else {
            glds16(W0 + (size_t)(kc * 64 + row) * ldw + bn * BN + panel * 128 + 8 * ch, slot + ASTG + q * 1024);
          }
        }
      }
    }
  };

#pragma unroll
  for (int s = 0; s < NS - 1; ++s) issue(s);

  if constexpr (AMODE == 1) {
    const int sub = tid & 7;
    constexpr int NP = BM / (NT / 8);
    u32x4 vv[NP][7];
#pragma unroll
    for (int p = 0; p < NP; ++p)
#pragma unroll
      for (int i = 0; i < 7; ++i) vv[p][i] = *(const u32x4*)(A + (size_t)(bm0 + p * (NT / 8) + (tid >> 3)) * KTOT + 8 * (sub + 8 * i));
    asm volatile("" ::: "memory");
#pragma unroll
    for (int p = 0; p < NP; ++p) {
      const int row = p * (NT / 8) + (tid >> 3);
      u32x4* v = vv[p];
      float s = 0.f;
#pragma unroll
      for (int i = 0; i < 7; ++i)
#pragma unroll
        for (int j = 0; j < 4; ++j) s += bflo(v[i][j]) + bfhi(v[i][j]);
      s += __shfl_xor(s, 1); s += __shfl_xor(s, 2); s += __shfl_xor(s, 4);
      const float mean = s * (1.f / KTOT);
      float q = 0.f;
#pragma unroll
      for (int i = 0; i < 7; ++i)
#pragma unroll
        for (int j = 0; j < 4; ++j) { const float a = bflo(v[i][j]) - mean, b = bfhi(v[i][j]) - mean; q += a * a + b * b; }
      q += __shfl_xor(q, 1); q += __shfl_xor(q, 2); q += __shfl_xor(q, 4);
      const float rstd = 1.f / sqrtf(q * (1.f / KTOT) + 1e-5f);
#pragma unroll
      for (int i = 0; i < 7; ++i) {
        u32x4 o;
#pragma unroll
        for (int j = 0; j < 4; ++j) o[j] = cvtpk((bflo(v[i][j]) - mean) * rstd, (bfhi(v[i][j]) - mean) * rstd);
        *(u32x4*)(Ares + row * PAR + 16 * (sub + 8 * i)) = o;
      }
    }
  }

  f32x16 acc[TM][TN];
#pragma unroll
  for (int i = 0; i < TM; ++i)
#pragma unroll
    for (int j = 0; j < TN; ++j) acc[i][j] = (f32x16){};

  const int tq = (lane & 15) >> 2, tp = lane & 3, tblk = (lane >> 4) & 1;
  bool swapped = true;
  if constexpr (EPI == 0) swapped = (bn < 14);

  auto compute = [&](auto sw_tag, int kc) {
    constexpr bool SW = decltype(sw_tag)::value;
    const char* slot = ring + (kc % NS) * STG;
    const char* Bb = slot + ASTG;
#pragma unroll
    for (int s = 0; s < 4; ++s) {
      bf16x8 af[TM], bfr[TN];
#pragma unroll
      for (int i = 0; i < TM; ++i) {
        const int row = wr * (BM / WM) + 32 * i + l32;
        if constexpr (AMODE == 1) af[i] = *(const bf16x8*)(Ares + row * PAR + (kc * 64 + 16 * s + 8 * hh) * 2);
        else af[i] = *(const bf16x8*)(slot + row * 128 + 16 * ((2 * s + hh) ^ swz_a128(row)));
      }
#pragma unroll
      for (int j = 0; j < TN; ++j) {
        int cb;
        if constexpr (BMODE == 2) cb = wc + 2 * j; else cb = wc * TN + j;
        const unsigned r0 = 16 * s + 8 * hh + tq;
        s16x4 lo, hi;
        if constexpr (BN == 64) {
          const unsigned ch = 4 * cb + 2 * tblk + (tp >> 1);
          lo = tr_read(Bb + 128 * r0 + 16 * (ch ^ swz_b128(r0)) + 8 * (tp & 1));
          hi = tr_read(Bb + 128 * (r0 + 4) + 16 * (ch ^ swz_b128(r0 + 4)) + 8 * (tp & 1));
        } else {
          const unsigned ch = 4 * (cb & 3) + 2 * tblk + (tp >> 1);
          const char* pb = Bb + (cb >> 2) * 16384;
          lo = tr_read(pb + 256 * r0 + 16 * (ch ^ swz_b256(r0)) + 8 * (tp & 1));
          hi = tr_read(pb + 256 * (r0 + 4) + 16 * (ch ^ swz_b256(r0 + 4)) + 8 * (tp & 1));
        }
        bfr[j] = (bf16x8){lo[0], lo[1], lo[2], lo[3], hi[0], hi[1], hi[2], hi[3]};
      }
      if constexpr (SW) {
#pragma unroll
        for (int i = 0; i < TM; ++i)
#pragma unroll
          for (int j = 0; j < TN; ++j) acc[i][j] = __builtin_amdgcn_mfma_f32_32x32x16_bf16(bfr[j], af[i], acc[i][j], 0, 0, 0);
      } else {
#pragma unroll
        for (int i = 0; i < TM; ++i)
#pragma unroll
          for (int j = 0; j < TN; ++j) acc[i][j] = __builtin_amdgcn_mfma_f32_32x32x16_bf16(af[i], bfr[j], acc[i][j], 0, 0, 0);
      }
    }
  };

  constexpr int STEADY = NK - (NS - 1);
  auto mainloop = [&](auto sw_tag) {
#pragma unroll 1
    for (int kc = 0; kc < STEADY; ++kc) {
      wait_bar<(NS - 2) * LPS>();
      issue(kc + NS - 1);
      compute(sw_tag, kc);
    }
#pragma unroll
    for (int kc = (STEADY > 0 ? STEADY : 0); kc < NK; ++kc) {
      wait_bar<0>();
      compute(sw_tag, kc);
    }
  };
  if (swapped) mainloop(std::integral_constant<bool, true>{});
  else mainloop(std::integral_constant<bool, false>{});

#pragma unroll
  for (int i = 0; i < TM; ++i) {
    const int tok0 = bm0 + wr * (BM / WM) + 32 * i;
    if constexpr (EPI == 1) {
#pragma unroll
      for (int j = 0; j < TN; ++j) {
        const int tok = tok0 + l32;
#pragma unroll
        for (int g = 0; g < 4; ++g) {
          const int n4 = bn * BN + (wc * TN + j) * 32 + 8 * g + 4 * hh;
          const u32x2 rv = *(const u32x2*)(R + (size_t)tok * DM + n4);
          u32x2 o;
          o.x = cvtpk(bflo(rv.x) + acc[i][j][4 * g], bfhi(rv.x) + acc[i][j][4 * g + 1]);
          o.y = cvtpk(bflo(rv.y) + acc[i][j][4 * g + 2], bfhi(rv.y) + acc[i][j][4 * g + 3]);
          *(u32x2*)(out0 + (size_t)tok * DM + n4) = o;
        }
      }
    } else if constexpr (EPI == 2) {
      const int tok = tok0 + l32;
#pragma unroll
      for (int g = 0; g < 4; ++g) {
        const int f4 = bn * 64 + wc * 32 + 8 * g + 4 * hh;
        float hv[4];
#pragma unroll
        for (int e = 0; e < 4; ++e) {
          const float gt = acc[i][0][4 * g + e], up = acc[i][1][4 * g + e];
          hv[e] = gt / (1.f + __expf(-gt)) * up;
        }
        u32x2 o; o.x = cvtpk(hv[0], hv[1]); o.y = cvtpk(hv[2], hv[3]);
        *(u32x2*)(out0 + (size_t)tok * FF + f4) = o;
      }
    } else {
      const int which = bn / 7, tile = tok0 >> 5;
#pragma unroll
      for (int j = 0; j < TN; ++j) {
        const int nb = (bn % 7) * 64 + (wc * TN + j) * 32;
        if (which < 2) {
          const int tok = tok0 + l32;
#pragma unroll
          for (int g = 0; g < 4; ++g) {
            const int n4 = nb + 8 * g + 4 * hh, head = n4 / DH, d = n4 - head * DH;
            u32x2 o;
            if (which == 0) {
              o.x = cvtpk(acc[i][j][4 * g] * QSCALE, acc[i][j][4 * g + 1] * QSCALE);
              o.y = cvtpk(acc[i][j][4 * g + 2] * QSCALE, acc[i][j][4 * g + 3] * QSCALE);
              u16* qp = out0 + ((size_t)head * SEQ + tok) * 32 + d;
              *(u32x2*)qp = o;
              if (d == 24) *(u32x2*)(qp + 4) = (u32x2){0u, 0u};
            } else {
              o.x = cvtpk(acc[i][j][4 * g], acc[i][j][4 * g + 1]);
              o.y = cvtpk(acc[i][j][4 * g + 2], acc[i][j][4 * g + 3]);
              char* kp = out1 + (size_t)head * 262144 + (size_t)tile * 4096 + (d >> 4) * 1024 + (l32 + 32 * ((d >> 3) & 1)) * 16 + 2 * (d & 7);
              *(u32x2*)kp = o;
              if (d == 24) *(u32x2*)(kp + 8) = (u32x2){0x3f80u, 0u};
            }
          }
        } else {
          const int n = nb + l32, head = n / DH, d = n - head * DH;
          char* vb = out1 + (size_t)head * 262144 + (size_t)tile * 4096 + 2048;
#pragma unroll
          for (int g = 0; g < 4; ++g) {
            u32x2 o;
            o.x = cvtpk(acc[i][j][4 * g], acc[i][j][4 * g + 1]);
            o.y = cvtpk(acc[i][j][4 * g + 2], acc[i][j][4 * g + 3]);
            char* vp = vb + (g >> 1) * 1024 + (d + 32 * hh) * 16 + 8 * (g & 1);
            *(u32x2*)vp = o;
            if (d == 27) {
              *(u32x2*)(vp + 16) = (u32x2){0x3f803f80u, 0x3f803f80u};
              *(u32x2*)(vp + 32) = (u32x2){0u, 0u};
              *(u32x2*)(vp + 48) = (u32x2){0u, 0u};
              *(u32x2*)(vp + 64) = (u32x2){0u, 0u};
            }
          }
        }
      }
    }
  }
}

constexpr float ATHR = 8.f;
template <bool FIRST>
__device__ __forceinline__ void attn_tile(const char* tb, int lane, bf16x8 qf0, bf16x8& qf1, float& mapp, f32x16& o) {
  const int hh = lane >> 5;
  const bf16x8 k0 = *(const bf16x8*)(tb + lane * 16);
  const bf16x8 k1 = *(const bf16x8*)(tb + 1024 + lane * 16);
  f32x16 s = __builtin_amdgcn_mfma_f32_32x32x16_bf16(k0, qf0, (f32x16){}, 0, 0, 0);
  s = __builtin_amdgcn_mfma_f32_32x32x16_bf16(k1, qf1, s, 0, 0, 0);
  float t0 = fmaxf(fmaxf(s[0], s[1]), s[2]), t1 = fmaxf(fmaxf(s[3], s[4]), s[5]);
  t0 = fmaxf(fmaxf(t0, s[6]), s[7]); t1 = fmaxf(fmaxf(t1, s[8]), s[9]);
  t0 = fmaxf(fmaxf(t0, s[10]), s[11]); t1 = fmaxf(fmaxf(t1, s[12]), s[13]);
  t0 = fmaxf(fmaxf(t0, s[14]), s[15]);
  float tmax = fmaxf(t0, t1);
  {
    auto rr = __builtin_amdgcn_permlane32_swap(__float_as_uint(tmax), __float_as_uint(tmax), false, false);
    tmax = fmaxf(__uint_as_float(rr[0]), __uint_as_float(rr[1]));
  }
  if (FIRST || __builtin_expect(__any(tmax > ATHR), 0)) {
    const float mnew = FIRST ? bflo(cvtpk(tmax, tmax)) : bflo(cvtpk(mapp + fmaxf(tmax, 0.f), 0.f));
    const float delta = mnew - mapp;
    if (!FIRST) {
      const float alpha = __builtin_amdgcn_exp2f(-delta);
#pragma unroll
      for (int r = 0; r < 16; ++r) o[r] *= alpha;
    }
#pragma unroll
    for (int r = 0; r < 16; ++r) s[r] -= delta;
    mapp = mnew;
    if (hh) qf1[4] = (short)(cvtpk(-mnew, 0.f) & 0xffffu);
  }
#pragma unroll
  for (int r = 0; r < 16; ++r) s[r] = __builtin_amdgcn_exp2f(s[r]);
  u32x4 p0, p1;
#pragma unroll
  for (int r = 0; r < 4; ++r) { p0[r] = cvtpk(s[2 * r], s[2 * r + 1]); p1[r] = cvtpk(s[8 + 2 * r], s[9 + 2 * r]); }
  const bf16x8 v0 = *(const bf16x8*)(tb + 2048 + lane * 16);
  const bf16x8 v1 = *(const bf16x8*)(tb + 3072 + lane * 16);
  o = __builtin_amdgcn_mfma_f32_32x32x16_bf16(v0, __builtin_bit_cast(bf16x8, p0), o, 0, 0, 0);
  o = __builtin_amdgcn_mfma_f32_32x32x16_bf16(v1, __builtin_bit_cast(bf16x8, p1), o, 0, 0, 0);
}

constexpr int ANS = 3;
__global__ __launch_bounds__(1024) void attn_k(const u16* __restrict__ Qs, const char* __restrict__ KV, u16* __restrict__ O) {
  extern __shared__ __attribute__((aligned(16))) char smem[];
  const int tid = threadIdx.x, lane = tid & 63, l32 = lane & 31, hh = lane >> 5;
  const int wid = __builtin_amdgcn_readfirstlane(tid >> 6), qt = wid & 3, kq = wid >> 2;
  const int head = (blockIdx.x & 7) * 2 + ((blockIdx.x >> 3) & 1), qb = blockIdx.x >> 4;
  const int q = qb * 128 + qt * 32 + l32;
  const char* kvh = KV + (size_t)head * 262144;
  auto stage = [&](int c) {
    char* buf = smem + (c % ANS) * 32768;
#pragma unroll
    for (int i = 0; i < 2; ++i) {
      const int p = i * 16 + wid;
      glds16(kvh + (size_t)(p >> 3) * 65536 + (size_t)c * 8192 + (p & 7) * 1024 + lane * 16, buf + p * 1024);
    }
  };
  bf16x8 qf0 = *(const bf16x8*)(Qs + ((size_t)head * SEQ + q) * 32 + 8 * hh);
  bf16x8 qf1 = *(const bf16x8*)(Qs + ((size_t)head * SEQ + q) * 32 + 16 + 8 * hh);
  asm volatile("s_waitcnt vmcnt(0)" : "+v"(qf0), "+v"(qf1)::"memory");
  stage(0);
  stage(1);
  f32x16 o = (f32x16){};
  float mapp = 0.f;
#pragma unroll 1
  for (int c = 0; c < 8; ++c) {
    if (c < 7) wait_bar<2>(); else wait_bar<0>();
    if (c + 2 < 8) stage(c + 2);
    const char* buf = smem + (c % ANS) * 32768 + kq * 8192;
    if (c == 0) attn_tile<true>(buf, lane, qf0, qf1, mapp, o);
    else attn_tile<false>(buf, lane, qf0, qf1, mapp, o);
    attn_tile<false>(buf + 4096, lane, qf0, qf1, mapp, o);
  }
  __syncthreads();
  float* mbuf = (float*)smem;
  if (kq > 0) {
    float* mb = mbuf + ((kq - 1) * 4 + qt) * 17 * 64;
#pragma unroll
    for (int r = 0; r < 16; ++r) mb[r * 64 + lane] = o[r];
    mb[16 * 64 + lane] = mapp;
  }
  __syncthreads();
  if (kq == 0) {
    float mk[3], mm = mapp;
#pragma unroll
    for (int k = 0; k < 3; ++k) { mk[k] = mbuf[((k * 4 + qt) * 17 + 16) * 64 + lane]; mm = fmaxf(mm, mk[k]); }
    const float a0 = __builtin_amdgcn_exp2f(mapp - mm);
#pragma unroll
    for (int r = 0; r < 16; ++r) o[r] *= a0;
#pragma unroll
    for (int k = 0; k < 3; ++k) {
      const float ak = __builtin_amdgcn_exp2f(mk[k] - mm);
#pragma unroll
      for (int r = 0; r < 16; ++r) o[r] += mbuf[((k * 4 + qt) * 17 + r) * 64 + lane] * ak;
    }
    float l = o[12];
    {
      auto rr = __builtin_amdgcn_permlane32_swap(__float_as_uint(l), __float_as_uint(l), false, false);
      l = __uint_as_float(rr[1]);
    }
    const float rl = 1.f / l;
    u16* orow = O + (size_t)q * DM + head * DH;
#pragma unroll
    for (int g = 0; g < 4; ++g) {
      if (g == 3 && hh == 1) continue;
      u32x2 w;
      w.x = cvtpk(o[4 * g] * rl, o[4 * g + 1] * rl);
      w.y = cvtpk(o[4 * g + 2] * rl, o[4 * g + 3] * rl);
      *(u32x2*)(orow + 8 * g + 4 * hh) = w;
    }
  }
}

__global__ __launch_bounds__(64) void ln_k(const u16* __restrict__ x, u16* __restrict__ y) {
  const int tid = threadIdx.x, sub = tid & 7, row = blockIdx.x * 8 + (tid >> 3);
  const u16* src = x + (size_t)row * DM;
  u32x4 v[7];
#pragma unroll
  for (int i = 0; i < 7; ++i) v[i] = *(const u32x4*)(src + 8 * (sub + 8 * i));
  float s = 0.f;
#pragma unroll
  for (int i = 0; i < 7; ++i)
#pragma unroll
    for (int j = 0; j < 4; ++j) s += bflo(v[i][j]) + bfhi(v[i][j]);
  s += __shfl_xor(s, 1); s += __shfl_xor(s, 2); s += __shfl_xor(s, 4);
  const float mean = s * (1.f / DM);
  float q = 0.f;
#pragma unroll
  for (int i = 0; i < 7; ++i)
#pragma unroll
    for (int j = 0; j < 4; ++j) { const float a = bflo(v[i][j]) - mean, b = bfhi(v[i][j]) - mean; q += a * a + b * b; }
  q += __shfl_xor(q, 1); q += __shfl_xor(q, 2); q += __shfl_xor(q, 4);
  const float rstd = 1.f / sqrtf(q * (1.f / DM) + 1e-5f);
#pragma unroll
  for (int i = 0; i < 7; ++i) {
    u32x4 o;
#pragma unroll
    for (int j = 0; j < 4; ++j) o[j] = cvtpk((bflo(v[i][j]) - mean) * rstd, (bfhi(v[i][j]) - mean) * rstd);
    *(u32x4*)(y + (size_t)row * DM + 8 * (sub + 8 * i)) = o;
  }
}

extern "C" void kernel_launch(void* const* d_in, const int* in_sizes, int n_in,
                              void* d_out, int out_size, void* d_ws, size_t ws_size,
                              hipStream_t stream) {
  const u16* x0 = (const u16*)d_in[0];
  const u16* wqkv = (const u16*)d_in[1];
  const u16* wo = (const u16*)d_in[2];
  const u16* w1 = (const u16*)d_in[4];
  const u16* w3 = (const u16*)d_in[5];
  const u16* w2 = (const u16*)d_in[6];
  char* ws = (char*)d_ws;
  u16* xa = (u16*)(ws);
  u16* xb = (u16*)(ws + (2u << 20));
  u16* Qs = (u16*)(ws + (4u << 20));
  char* KV = ws + (6u << 20);
  u16* Ob = (u16*)(ws + (10u << 20));
  u16* Hh = (u16*)(ws + (12u << 20));
  u16* out = (u16*)d_out;
  u16* Ln = (u16*)(ws + (20u << 20));

  constexpr int LDS_QKV = 3 * 24576;
  constexpr int LDS_GU = 2 * 32768;
  constexpr int LDS_P = 4 * 16384;
  auto k_qkv = gemm_k<128, 64, 448, 3, 2, 2, 0, 0, 0>;
  auto k_oproj = gemm_k<64, 64, 448, 4, 2, 2, 0, 0, 1>;
  auto k_gu = gemm_k<128, 128, 448, 2, 2, 2, 0, 2, 2>;
  auto k_down = gemm_k<64, 64, 1792, 4, 2, 2, 0, 0, 1>;
  (void)hipFuncSetAttribute((const void*)k_qkv, hipFuncAttributeMaxDynamicSharedMemorySize, LDS_QKV);
  (void)hipFuncSetAttribute((const void*)k_gu, hipFuncAttributeMaxDynamicSharedMemorySize, LDS_GU);
  (void)hipFuncSetAttribute((const void*)k_oproj, hipFuncAttributeMaxDynamicSharedMemorySize, LDS_P);
  (void)hipFuncSetAttribute((const void*)k_down, hipFuncAttributeMaxDynamicSharedMemorySize, LDS_P);
  (void)hipFuncSetAttribute((const void*)attn_k, hipFuncAttributeMaxDynamicSharedMemorySize, ANS * 32768);

  const u16* xin = x0;
  for (int layer = 0; layer < 2; ++layer) {
    u16* x1 = xa;
    u16* x2 = (layer == 0) ? xb : out;
    hipLaunchKernelGGL(ln_k, dim3(256), dim3(64), 0, stream, xin, Ln);
    hipLaunchKernelGGL(k_qkv, dim3(21 * 16), dim3(256), LDS_QKV, stream, (const u16*)Ln, wqkv, (const u16*)nullptr, (const u16*)nullptr, Qs, KV, 3 * DM, 21);
    hipLaunchKernelGGL(attn_k, dim3(256), dim3(1024), ANS * 32768, stream, Qs, (const char*)KV, Ob);
    hipLaunchKernelGGL(k_oproj, dim3(7 * 32), dim3(256), LDS_P, stream, (const u16*)Ob, wo, (const u16*)nullptr, xin, x1, (char*)nullptr, DM, 7);
    hipLaunchKernelGGL(ln_k, dim3(256), dim3(64), 0, stream, (const u16*)x1, Ln);
    hipLaunchKernelGGL(k_gu, dim3(28 * 16), dim3(256), LDS_GU, stream, (const u16*)Ln, w1, w3, (const u16*)nullptr, Hh, (char*)nullptr, FF, 28);
    hipLaunchKernelGGL(k_down, dim3(7 * 32), dim3(256), LDS_P, stream, (const u16*)Hh, w2, (const u16*)nullptr, (const u16*)x1, x2, (char*)nullptr, DM, 7);
    xin = x2;
  }
}
```
